```python
import torch

def _softmax(x, axis=-1):
    return torch.softmax(x, dim=axis)
def _gelu(x):
    return torch.nn.functional.gelu(x, approximate="tanh")
def _silu(x):
    return torch.nn.functional.silu(x)
def _sigmoid(x):
    return torch.sigmoid(x)
def _layernorm(x, axis=-1, eps=1e-5):
    m = x.mean(dim=axis, keepdim=True)
    v = x.var(dim=axis, keepdim=True, unbiased=False)
    return (x - m) / torch.sqrt(v + eps)
def _scatter_add(tgt, idx, src, axis=0):
    return tgt.clone().index_add_(axis, idx, src)
def _segment_sum(src, seg, n):
    out = torch.zeros((n,) + src.shape[1:], dtype=src.dtype)
    return out.index_add_(0, seg, src)
def _pad(x, pw):
    flat = tuple(v for pair in reversed(pw) for v in pair)
    return torch.nn.functional.pad(x, flat)

kernel_name = 'gated_mix__linear_attn_block__glu_gated_swiglu__prompt'

def setup_inputs():

    _w = (lambda a: (lambda t: t.to(torch.bfloat16) if t.is_floating_point() else t)(torch.as_tensor(a)))
    def _A_setup():
        B, S, D = 1, 2048, 448
        return {
            "x": _w((lambda *s: torch.randn(*s, dtype=torch.bfloat16) * 0.05)(B, S, D)),
            "wqkv": _w((lambda *s: torch.randn(*s, dtype=torch.bfloat16) * 0.8)(D, 3 * D)),
            "wo": _w((lambda *s: torch.randn(*s, dtype=torch.bfloat16) * 0.05)(D, D)),
        }
    _A_in = _A_setup()

    def _B_setup():
        B, S, D, F = 1, 2048, 448, 1792
        return {
            "x": _w((lambda *s: torch.randn(*s, dtype=torch.bfloat16) * 0.05)(B, S, D)),
            "w1": _w((lambda *s: torch.randn(*s, dtype=torch.bfloat16) * 1.6)(D, F) * (2.0 / D) ** 0.5),
            "w3": _w((lambda *s: torch.randn(*s, dtype=torch.bfloat16) * 1.6)(D, F) * (2.0 / D) ** 0.5),
            "w2": _w((lambda *s: torch.randn(*s, dtype=torch.bfloat16) * 1.6)(F, D) * (2.0 / F) ** 0.5),
        }
    _B_in = _B_setup()

    return {
        "A_x": _A_in["x"],
        "A_wqkv": _A_in["wqkv"],
        "A_wo": _A_in["wo"],
        "B_x": _B_in["x"],
        "B_w1": _B_in["w1"],
        "B_w3": _B_in["w3"],
        "B_w2": _B_in["w2"],
        "gate_w": _w((lambda *s: torch.randn(*s, dtype=torch.bfloat16) * 0.05)(448) * 16.0),
    }

def reference(A_x, A_wqkv, A_wo, B_x, B_w1, B_w3, B_w2, gate_w):
    def _A_ref(x, wqkv, wo):
        B, S, D = x.shape
        H = 16; Dh = D // H
        qkv = (lambda x, *d: x.permute(*d))((x @ wqkv).reshape(B, S, 3, H, Dh), 2, 0, 3, 1, 4)
        q, k, v = qkv[0], qkv[1], qkv[2]
        phi = lambda t: torch.where(t > 0, t + 1, torch.exp(t)) + 1e-6
        pq, pk = phi(q), phi(k)
        kv = torch.einsum("bhsd,bhse->bhde", pk, v)
        z = torch.einsum("bhsd->bhd", pk)
        num = torch.einsum("bhqd,bhde->bhqe", pq, kv)
        den = torch.einsum("bhqd,bhd->bhq", pq, z)[..., None] + 1e-6
        o = num / den
        return (lambda x, *d: x.permute(*d))(o, 0, 2, 1, 3).reshape(B, S, D) @ wo

    def _B_ref(x, w1, w3, w2):
        gate = torch.einsum("bsd,df->bsf", x, w1)
        up = torch.einsum("bsd,df->bsf", x, w3)
        h = _silu(gate) * up
        out = torch.einsum("bsf,fd->bsd", h, w2)
        return out

    x = A_x
    g = _sigmoid((x * gate_w).sum(-1, keepdims=True))
    a = _A_ref(x, A_wqkv, A_wo)
    b = _B_ref(x, B_w1, B_w3, B_w2)
    return g * a + (1 - g) * b
```

```cpp
#include <hip/hip_runtime.h>
#include <hip/hip_bf16.h>
#include <stdint.h>

typedef __attribute__((ext_vector_type(8))) short bf16x8;
typedef __attribute__((ext_vector_type(4))) short s16x4;
typedef __attribute__((ext_vector_type(16))) float f32x16;
typedef __attribute__((ext_vector_type(4))) float f32x4;
typedef __attribute__((ext_vector_type(4))) unsigned u32x4;
typedef __attribute__((ext_vector_type(2))) unsigned u32x2;
typedef __attribute__((address_space(3))) s16x4* lds_s16x4_ptr;
typedef __attribute__((address_space(3))) bf16x8* lds_bf16x8_ptr;
typedef __attribute__((address_space(3))) char* lds_char_ptr;
typedef __attribute__((address_space(1))) unsigned gu32;
typedef unsigned short u16;

#define DI __device__ __forceinline__
#define MFMA(a, b, c) __builtin_amdgcn_mfma_f32_32x32x16_bf16((a), (b), (c), 0, 0, 0)
#define RLX_AGENT __ATOMIC_RELAXED, __HIP_MEMORY_SCOPE_AGENT

constexpr int S_ = 2048, D_ = 448, F_ = 1792, NQKV = 1344;

constexpr size_t WS_HF = 0;
constexpr size_t WS_PQ = WS_HF + (size_t)64 * 112 * 1024;
constexpr size_t WS_KVP = WS_PQ + (size_t)64 * 16 * 2048;
constexpr size_t WS_KVT = WS_KVP + (size_t)32 * 16 * 4096;
constexpr size_t WS_OF = WS_KVT + 32768;
constexpr size_t WS_G = WS_OF + (size_t)64 * 16 * 2048;
constexpr size_t WS_CNT = WS_G + 8192;

#define STAMP_DECL
#define STAMP_PARAM
#define STAMP_ARG
#define STAMP_NOW(i)
#define STAMP_OUT(ws, kidx, n)
template <int N> DI void wait_vm() { asm volatile("s_waitcnt vmcnt(%0)" ::"n"(N) : "memory"); }
DI void wait_lgkm0() { asm volatile("s_waitcnt lgkmcnt(0)" ::: "memory"); }

typedef __attribute__((ext_vector_type(2))) __bf16 bf16x2v;
typedef __attribute__((ext_vector_type(2))) float f32x2;
DI unsigned pack2(float lo, float hi) {
  f32x2 f = {lo, hi};
  bf16x2v v = __builtin_convertvector(f, bf16x2v);
  return __builtin_bit_cast(unsigned, v);
}
DI float bf16r(float x) { return (float)(__bf16)x; }
DI void glds16(const void* g, lds_char_ptr l) {
  const unsigned la = __builtin_amdgcn_readfirstlane((unsigned)(uintptr_t)l);
  asm volatile("s_mov_b32 m0, %1\n\ts_nop 0\n\tglobal_load_lds_dwordx4 %0, off" ::"v"(g), "s"(la));
}
DI void glds16s(unsigned voff, const void* sbase, lds_char_ptr l) {
  const unsigned la = __builtin_amdgcn_readfirstlane((unsigned)(uintptr_t)l);
  asm volatile("s_mov_b32 m0, %2\n\ts_nop 0\n\tglobal_load_lds_dwordx4 %0, %1" ::"v"(voff), "s"(sbase), "s"(la));
}
DI void glds16s_sc1(unsigned voff, const void* sbase, lds_char_ptr l) {
  const unsigned la = __builtin_amdgcn_readfirstlane((unsigned)(uintptr_t)l);
  asm volatile("s_mov_b32 m0, %2\n\ts_nop 0\n\tglobal_load_lds_dwordx4 %0, %1 sc1" ::"v"(voff), "s"(sbase), "s"(la));
}
DI void glds16s48(unsigned voff, const void* sbase, lds_char_ptr l) {
  const unsigned la = __builtin_amdgcn_readfirstlane((unsigned)(uintptr_t)l);
  asm volatile("s_mov_b32 m0, %2\n\ts_mov_b32 exec_hi, 0xffff\n\tglobal_load_lds_dwordx4 %0, %1\n\ts_mov_b32 exec_hi, -1"
               ::"v"(voff), "s"(sbase), "s"(la));
}
DI void l2touch_s(unsigned voff, const void* sbase, lds_char_ptr junk) {
  const unsigned la = __builtin_amdgcn_readfirstlane((unsigned)(uintptr_t)junk);
  asm volatile("s_mov_b32 m0, %2\n\ts_nop 0\n\tglobal_load_lds_dword %0, %1" ::"v"(voff), "s"(sbase), "s"(la));
}
DI void l2touch(const void* g, lds_char_ptr junk) {
  const unsigned la = __builtin_amdgcn_readfirstlane((unsigned)(uintptr_t)junk);
  asm volatile("s_mov_b32 m0, %1\n\ts_nop 0\n\tglobal_load_lds_dword %0, off" ::"v"(g), "s"(la));
}
#define STPOL "sc1"
DI void gstore16(void* p, u32x4 v) { asm volatile("global_store_dwordx4 %0, %1, off " STPOL "\n\ts_nop 1" ::"v"(p), "v"(v) : "memory"); }
DI s16x4 trread(lds_char_ptr p) { return __builtin_amdgcn_ds_read_tr16_b64_v4i16((lds_s16x4_ptr)p); }
DI bf16x8 ldsread16(lds_char_ptr p) { return *(lds_bf16x8_ptr)p; }
DI bf16x8 cat8(s16x4 lo, s16x4 hi) { return __builtin_shufflevector(lo, hi, 0, 1, 2, 3, 4, 5, 6, 7); }
DI float fast_exp(float x) { return __builtin_amdgcn_exp2f(x * 1.4426950408889634f); }
DI float fast_rcp(float x) { return __builtin_amdgcn_rcpf(x); }
DI float phi(float t) {
  const float e = __builtin_amdgcn_fmed3f(__builtin_amdgcn_exp2f(t * 1.4426950408889634f), 0.f, 1.f);
  return (fmaxf(t, 0.f) + 1e-6f) + e;
}

enum { T_F = 0, T_Q = 1, T_K = 2, T_V = 3 };

DI void wg_barrier() {
  asm volatile("s_waitcnt lgkmcnt(0)" ::: "memory");
  __builtin_amdgcn_s_barrier();
  asm volatile("" ::: "memory");
}

DI void tile_info(int cg, int t, int& type, int& idx) {
  if (cg < 14) {
    if (t < 8) { type = T_F; idx = cg * 8 + t; }
    else if (cg < 8) { type = (t == 8) ? T_K : T_V; idx = cg; }
    else { type = T_Q; idx = 2 * (cg - 8) + (t - 8); }
  } else {
    int c = t >= 5 ? 1 : 0, tl = t - 5 * c;
    if (tl == 0) { type = T_Q; idx = 12 + 2 * (cg - 14) + c; }
    else { type = (tl & 1) ? T_K : T_V; idx = 8 + 4 * (cg - 14) + 2 * c + ((tl - 1) >> 1); }
  }
}
DI int qkv_col(int cg, int t) {
  int type, idx;
  if (cg < 8) { type = (t == 8) ? T_K : T_V; idx = cg; }
  else if (cg < 14) { type = T_Q; idx = 2 * (cg - 8) + (t - 8); }
  else {
    const int c = t >= 5 ? 1 : 0, tl = t - 5 * c;
    type = tl == 0 ? T_Q : ((tl & 1) ? T_K : T_V);
    idx = tl == 0 ? 12 + 2 * (cg - 14) + c : 8 + 4 * (cg - 14) + 2 * c + ((tl - 1) >> 1);
  }
  return idx * 28 + (type == T_Q ? 0 : (type == T_K ? 448 : 892));
}

constexpr int K1_XOFF = 0;
constexpr int K1_WOFF = 3 * 16384;
constexpr int K1_WSLOT = 20480;
constexpr int K1_JUNK = K1_WOFF + 4 * K1_WSLOT;
constexpr int K1_FLAG = K1_JUNK + 256;
constexpr int K1_LDS = K1_FLAG + 16;
#define PFAT 1
#define WARM_AT 3
#define ROTK 7
constexpr int K1_PD = 7;

struct K1State {
  const char* bF[2];
  const char* bE;
  const char* bX;
  unsigned oF[4], oE, oX[2];
  int strideF;
  int rot;
  const char* warm[2];
};
DI int k1_chunk(const K1State& st, int c) { const int ch = c + st.rot; return ch >= 7 ? ch - 7 : ch; }

template <int I>
DI void k1_issue(const K1State& st, lds_char_ptr lds, int w) {
  lds_char_ptr wb = lds + K1_WOFF + (I & 3) * K1_WSLOT;
  const int kb = 2 * k1_chunk(st, I >> 1) + (I & 1);
#pragma unroll
  for (int j = 0; j < 4; ++j) glds16s(st.oF[j], st.bF[j & 1] + kb * st.strideF, wb + (4 * w + j) * 1024);
  glds16s(st.oE, st.bE + kb * (32 * NQKV * 2), wb + 16384 + w * 1024);
}
template <int J>
DI void k1_issue_x(const K1State& st, lds_char_ptr lds, int w) {
  lds_char_ptr xb = lds + K1_XOFF + ((J >> 1) % 3) * 16384 + (J & 1) * 8192;
#pragma unroll
  for (int j = 0; j < 2; ++j) glds16s(st.oX[j], st.bX + (J & 1) * 64 * D_ * 2 + k1_chunk(st, J >> 1) * 128, xb + (2 * w + j) * 1024);
}
template <int I>
DI void k1_issue_group(const K1State& st, lds_char_ptr lds, int w) {
  if constexpr (I < 14) {
    k1_issue<I>(st, lds, w);
    if constexpr (I + 1 < 14) k1_issue_x<I + 1>(st, lds, w);
  }
}

template <int I>
DI void k1_loader_step(const K1State& st, lds_char_ptr lds, int lw STAMP_PARAM) {
  constexpr int outstanding = (I == 0) ? 7 : (I <= 10) ? 14 : (I == 11 ? 12 : (I == 12 ? 5 : 0));
  if constexpr (I < 8) STAMP_NOW(4 + 3 * I);
  wait_vm<outstanding>();
  if constexpr (I < 8) STAMP_NOW(5 + 3 * I);
  wg_barrier();
  if constexpr (I < 8) STAMP_NOW(6 + 3 * I);
  if constexpr (I == 0) { k1_issue_group<2>(st, lds, lw); k1_issue_group<3>(st, lds, lw); }
  else k1_issue_group<I + 3>(st, lds, lw);
  if constexpr (I >= WARM_AT && I < WARM_AT + 8) {
    if (lw == ((I - WARM_AT) & 3)) l2touch(st.warm[(I - WARM_AT) >> 2], lds + K1_JUNK);
  }
  if constexpr (I + 1 < 14) k1_loader_step<I + 1>(st, lds, lw STAMP_ARG);
}

template <int NT> struct K1Frag { bf16x8 x[2]; bf16x8 w[NT]; };
template <int NT> struct K1Addr {
  int x; int w[NT][4];
  const char* tb; unsigned to; int t4;
};
template <int NT, int B>
DI void k1_touch(const K1Addr<NT>& ad, lds_char_ptr lds) {
}

template <int NT, int I, int KS>
DI void k1_read(K1Frag<NT>& f, lds_char_ptr lds, const K1Addr<NT>& ad) {
  constexpr int kk = (I & 1) * 4 + KS * 2;
  lds_char_ptr xb = lds + K1_XOFF + ((I >> 1) % 3) * 16384;
  f.x[0] = ldsread16(xb + (ad.x ^ (kk << 4)));
  f.x[1] = ldsread16(xb + (ad.x ^ (kk << 4)) + 4096);
  lds_char_ptr wb = lds + K1_WOFF + (I & 3) * K1_WSLOT;
#pragma unroll
  for (int t = 0; t < NT; ++t) {
    s16x4 lo, hi;
    if constexpr (NT == 3) {
      lo = trread(wb + ad.w[t][0] + (4 * KS) * 2048);
      hi = trread(wb + ad.w[t][0] + (4 * KS + 1) * 2048);
    } else {
      lo = trread(wb + ad.w[t][2 * KS]);
      hi = trread(wb + ad.w[t][2 * KS + 1]);
    }
    f.w[t] = cat8(lo, hi);
  }
}
template <int NT>
DI void k1_mma(f32x16 (&acc)[NT][2], const K1Frag<NT>& f) {
#pragma unroll
  for (int t = 0; t < NT; ++t) {
    acc[t][0] = MFMA(f.w[t], f.x[0], acc[t][0]);
    acc[t][1] = MFMA(f.w[t], f.x[1], acc[t][1]);
  }
}

template <int NT, int I>
DI void k1_step(f32x16 (&acc)[NT][2], K1Frag<NT>& fa, K1Frag<NT>& fb, lds_char_ptr lds, const K1Addr<NT>& ad STAMP_PARAM) {
  wg_barrier();
  STAMP_NOW(2 + I);
  if constexpr (I == PFAT) { k1_touch<NT, 2>(ad, lds); k1_touch<NT, 3>(ad, lds); }
  k1_read<NT, I, 0>(fa, lds, ad);
  if constexpr (I > 0) k1_mma<NT>(acc, fb);
  k1_read<NT, I, 1>(fb, lds, ad);
  k1_mma<NT>(acc, fa);
  if constexpr (I + 1 < 14) k1_step<NT, I + 1>(acc, fa, fb, lds, ad STAMP_ARG);
}

template <int NT>
DI void k1_compute(lds_char_ptr lds, char* __restrict__ ws, int w, int lane, int rt, int cg, int bi, const u16* x, const u16* wqkv,
                   const u16* w1, const u16* w3 STAMP_PARAM) {
  const int r = w & 1, c = (w >> 1) & 1;
  const int t0 = 5 * c + (NT == 3 ? 0 : 3);
  const int hh = lane >> 5, m = lane & 31;
  const int q4 = (lane & 15) >> 2, p4 = lane & 3, blk2 = (lane >> 4) & 1;
  const int xrow = 64 * r + m;
  K1Addr<NT> ad;
  {
    const int sel = w & 3, half = w >> 2, sg = (lane >> 3) & 3;
    if (sel == 3) {
      ad.tb = (const char*)(x + (size_t)(rt * 128 + (bi >> 3) * 32) * D_);
      ad.to = ((sg & 1) * 16 + half * 8 + (lane & 7)) * (D_ * 2) + (sg >> 1) * 128;
      ad.t4 = 256;
    } else {
      const int row = 32 * sg + 4 * (bi & 7) + 2 * half + (lane & 1), j = (lane >> 1) & 3;
      if (cg < 14 && sel < 2) {
        ad.tb = (const char*)(sel ? w3 : w1);
        ad.to = (row * F_ + 128 * cg + 64 * (j & 1)) * 2;
        ad.t4 = 128 * F_ * 2;
      } else {
        int col;
        if (cg < 14) col = qkv_col(cg, 8 + (j >> 1)) + 30 * (j & 1);
        else col = sel == 2 ? qkv_col(cg, 0) + 58 * (j & 1) : qkv_col(cg, 1 + sel) + 56 * (j < 3 ? j : 2);
        ad.tb = (const char*)wqkv;
        ad.to = (row * NQKV + col) * 2;
        ad.t4 = 128 * NQKV * 2;
      }
    }
  }
  ad.x = xrow * 128 + ((hh ^ ((xrow >> 1) & 7)) << 4);
  const int inblk = q4 * 64 + blk2 * 32 + p4 * 8;
#pragma unroll
  for (int t = 0; t < NT; ++t) {
    const int ts = t0 + t;
    int off, kst;
    if (ts >= 8) { off = 16384 + (ts - 8) * 256 + inblk; kst = 512; }
    else if (cg >= 14) { off = ts * 256 + inblk; kst = 2048; }
    else { off = ((ts >> 1) + 4 * blk2) * 256 + q4 * 64 + (((ts & 1) ^ blk2) * 32) + p4 * 8; kst = 2048; }
#pragma unroll
    for (int i = 0; i < 4; ++i) ad.w[t][i] = off + (4 * (i >> 1) + (i & 1) + 2 * hh) * kst;
  }

  f32x16 acc[NT][2];
#pragma unroll
  for (int t = 0; t < NT; ++t)
#pragma unroll
    for (int s = 0; s < 2; ++s)
#pragma unroll
      for (int i = 0; i < 16; ++i) acc[t][s][i] = 0.f;
  K1Frag<NT> fa, fb;
  if (w == 0 && lane < 4) *((__attribute__((address_space(3))) unsigned*)(lds + K1_FLAG) + lane) = 0u;
  STAMP_NOW(1);
  k1_step<NT, 0>(acc, fa, fb, lds, ad STAMP_ARG);
  k1_mma<NT>(acc, fb);
  STAMP_NOW(16);

  const int strip0 = rt * 4 + r * 2;
  {
    int type, idx; tile_info(cg, t0 + NT - 2, type, idx);
    if (type == T_K) {
      __builtin_amdgcn_s_setprio(3);
      lds_char_ptr img = lds + K1_WOFF + 2 * K1_WSLOT + w * 4096;
      constexpr int tk = NT - 2, tv = NT - 1;
      f32x16 kv;
#pragma unroll
      for (int i = 0; i < 16; ++i) kv[i] = 0.f;
#pragma unroll
      for (int s = 0; s < 2; ++s) {
#pragma unroll
        for (int g = 0; g < 4; ++g) {
          u32x2 pk = {pack2(phi(acc[tk][s][4 * g]), phi(acc[tk][s][4 * g + 1])), pack2(phi(acc[tk][s][4 * g + 2]), phi(acc[tk][s][4 * g + 3]))};
          float v0 = acc[tv][s][4 * g];
          if (g == 0) v0 = hh == 0 ? 1.f : v0;
          u32x2 vv = {pack2(v0, acc[tv][s][4 * g + 1]), pack2(acc[tv][s][4 * g + 2], acc[tv][s][4 * g + 3])};
          const int off = m * 64 + (((2 * g + hh) ^ ((m >> 1) & 7)) << 3);
          *(__attribute__((address_space(3))) u32x2*)(img + off) = pk;
          *(__attribute__((address_space(3))) u32x2*)(img + 2048 + off) = vv;
        }
#pragma unroll
        for (int ks = 0; ks < 2; ++ks) {
          const int r0 = 16 * ks + 8 * hh + q4, r1 = r0 + 4, un = 4 * blk2 + p4;
          const int off0 = r0 * 64 + ((un ^ ((r0 >> 1) & 7)) << 3), off1 = r1 * 64 + ((un ^ ((r1 >> 1) & 7)) << 3);
          bf16x8 a = cat8(trread(img + off0), trread(img + off1));
          bf16x8 bb = cat8(trread(img + 2048 + off0), trread(img + 2048 + off1));
          kv = MFMA(a, bb, kv);
        }
      }
      lds_char_ptr xch = lds + K1_XOFF + 16384 + (w >> 1) * 4096;
      volatile __attribute__((address_space(3))) unsigned* flag = (volatile __attribute__((address_space(3))) unsigned*)(lds + K1_FLAG) + (w >> 1);
      if (r == 1) {
#pragma unroll
        for (int g = 0; g < 4; ++g) {
          f32x4 v = {kv[4 * g], kv[4 * g + 1], kv[4 * g + 2], kv[4 * g + 3]};
          *(__attribute__((address_space(3))) f32x4*)(xch + g * 1024 + lane * 16) = v;
        }
        wait_lgkm0();
        *flag = 1u;
      } else {
        while (__builtin_amdgcn_readfirstlane(*flag) == 0u) __builtin_amdgcn_s_sleep(1);
        asm volatile("" ::: "memory");
#pragma unroll
        for (int g = 0; g < 4; ++g) {
          f32x4 v = *(__attribute__((address_space(3))) f32x4*)(xch + g * 1024 + lane * 16);
          v[0] += kv[4 * g]; v[1] += kv[4 * g + 1]; v[2] += kv[4 * g + 2]; v[3] += kv[4 * g + 3];
          gstore16(ws + WS_KVP + ((size_t)(rt * 16 + idx) * 4 + g) * 1024 + lane * 16, __builtin_bit_cast(u32x4, v));
        }
      }
      __builtin_amdgcn_s_setprio(0);
    }
  }
#pragma unroll
  for (int t = 0; t < NT; ++t) {
    int type, idx; tile_info(cg, t0 + t, type, idx);
    if (type == T_F) {
#pragma unroll
      for (int s = 0; s < 2; ++s) {
        float hv[8];
#pragma unroll
        for (int j = 0; j < 8; ++j) {
          const float gt = acc[t][s][j], up = acc[t][s][8 + j];
          hv[j] = gt * up * fast_rcp(1.f + fast_exp(-gt));
        }
        u32x4 o = {pack2(hv[0], hv[1]), pack2(hv[2], hv[3]), pack2(hv[4], hv[5]), pack2(hv[6], hv[7])};
        gstore16(ws + WS_HF + ((size_t)(strip0 + s) * 112 + idx) * 1024 + lane * 16, o);
      }
    } else if (type == T_Q) {
#pragma unroll
      for (int s = 0; s < 2; ++s) {
        float pv[16];
#pragma unroll
        for (int j = 0; j < 16; ++j) pv[j] = phi(acc[t][s][j]);
        if (hh) { pv[12] = 0.f; pv[13] = 0.f; pv[14] = 0.f; pv[15] = 0.f; }
        u32x4 o0 = {pack2(pv[0], pv[1]), pack2(pv[2], pv[3]), pack2(pv[4], pv[5]), pack2(pv[6], pv[7])};
        u32x4 o1 = {pack2(pv[8], pv[9]), pack2(pv[10], pv[11]), pack2(pv[12], pv[13]), pack2(pv[14], pv[15])};
        char* dst = ws + WS_PQ + ((size_t)(strip0 + s) * 16 + idx) * 2048 + lane * 16;
        gstore16(dst, o0);
        gstore16(dst + 1024, o1);
      }
    }
  }
  STAMP_NOW(17);
  STAMP_NOW(18);
  if (w >= 2 && w < 6) STAMP_OUT(ws, 0, 19);
}

__global__ __launch_bounds__(768, 3) void k1_kernel(const u16* __restrict__ x, const u16* __restrict__ wqkv, const u16* __restrict__ w1,
                                                    const u16* __restrict__ w3, const u16* __restrict__ gw, const u16* __restrict__ w2,
                                                    const u16* __restrict__ wo, char* __restrict__ ws, const u16* __restrict__ out) {
  __shared__ __attribute__((aligned(16))) char smem[K1_LDS];
  lds_char_ptr lds = (lds_char_ptr)smem;
  const int tid = threadIdx.x, lane = tid & 63;
  const int w = __builtin_amdgcn_readfirstlane(tid >> 6);
  STAMP_DECL
  STAMP_NOW(0);
  const int b = blockIdx.x, xcd = b & 7, bi = b >> 3;
  const int rt = (xcd & 1) * 8 + (bi & 7);
  const int cg = (xcd >> 1) * 4 + (bi >> 3);

  asm volatile("" ::"s"(x), "s"(wqkv), "s"(w1), "s"(w3));
  if (__builtin_expect(w >= 8, 1)) {
    const int lw = w - 8;
    K1State st;
    const int blk = lane >> 4, row = (lane >> 2) & 3, u = lane & 3;
    st.bX = (const char*)(x + (size_t)rt * 128 * D_);
    st.rot = ROTK == 7 ? ((bi & 7) + 2 * (bi >> 3)) % 7 : ((((bi & 7) + (bi >> 3)) % ROTK) * 7) / ROTK;
#pragma unroll
    for (int j = 0; j < 2; ++j) {
      const int xr = 8 * (2 * lw + j) + (lane >> 3), pos = lane & 7, ch = pos ^ ((xr >> 1) & 7);
      st.oX[j] = (xr * D_ + ch * 8) * 2;
    }
    STAMP_NOW(1);
    k1_issue_x<0>(st, lds, lw);
    STAMP_NOW(2);
    st.bE = (const char*)wqkv;
    if (cg < 14) {
      st.strideF = 32 * F_ * 2;
      st.bF[0] = (const char*)w1; st.bF[1] = (const char*)w3;
#pragma unroll
      for (int j = 0; j < 4; ++j)
        st.oF[j] = ((4 * (2 * lw + (j >> 1)) + row) * F_ + 128 * cg + 32 * blk + 8 * ((j & 1) ? (u ^ 2) : u)) * 2;
    } else {
      st.strideF = 32 * NQKV * 2;
      st.bF[0] = (const char*)wqkv; st.bF[1] = (const char*)wqkv;
#pragma unroll
      for (int j = 0; j < 4; ++j) st.oF[j] = ((4 * (2 * lw + (j >> 1)) + row) * NQKV + qkv_col(cg, 4 * (j & 1) + blk) + 8 * u) * 2;
    }
    st.oE = ((4 * (2 * lw + (blk >> 1)) + row) * NQKV + qkv_col(cg, 8 + (blk & 1)) + 8 * u) * 2;
    STAMP_NOW(3);
    k1_issue_group<0>(st, lds, lw);
    k1_issue_group<1>(st, lds, lw);
    {
      const int l2 = bi * 392 + lw * 98;
      int lo = bi * 98 + lw * 25 + (lane - 34);
      lo = lo < 3136 ? lo : 3135;
      st.warm[0] = (const char*)w2 + (size_t)(l2 + lane) * 128;
      st.warm[1] = lane < 34 ? (const char*)w2 + (size_t)(l2 + 64 + lane) * 128 : (const char*)wo + (size_t)(lane < 59 ? lo : bi * 98) * 128;
    }
    k1_loader_step<0>(st, lds, lw STAMP_ARG);
    if (lw == 0) {
      if (b == 0 && lane < 32) *(unsigned*)(ws + WS_CNT + lane * 256) = 0u;
      const int grow = rt * 128 + cg * 8 + (lane >> 3), seg = lane & 7;
      u32x4 xv[7], gv[7];
#pragma unroll
      for (int i = 0; i < 7; ++i) {
        xv[i] = *(const u32x4*)(x + (size_t)grow * D_ + seg * 56 + i * 8);
        gv[i] = *(const u32x4*)(gw + seg * 56 + i * 8);
      }
      float gsum = 0.f;
#pragma unroll
      for (int i = 0; i < 7; ++i)
#pragma unroll
        for (int j = 0; j < 4; ++j) {
          const unsigned xe = xv[i][j], ge = gv[i][j];
          gsum = __builtin_amdgcn_fdot2_f32_bf16(__builtin_bit_cast(bf16x2v, xe), __builtin_bit_cast(bf16x2v, ge), gsum, false);
        }
      gsum += __shfl_xor(gsum, 1);
      gsum += __shfl_xor(gsum, 2);
      gsum += __shfl_xor(gsum, 4);
      const float gs = bf16r(gsum);
      const float g = bf16r(1.f / (1.f + __expf(-gs)));
      if (seg == 0) *(float*)(ws + WS_G + grow * 4) = g;
    }
    STAMP_OUT(ws, 2, 28);
    return;
  }
  if (w == 7) {
    const char* pc; int len;
    asm volatile("s_getpc_b64 %0\nk1_code_mark:\n\ts_mov_b32 %1, k2_code_mark-k1_code_mark" : "=s"(pc), "=s"(len));
    int off = (bi * 16 + (lane & 15)) * 128;
    off = off < len ? off : 0;
    l2touch(pc + off, lds + K1_JUNK);
  }
  if (w < 4) k1_compute<3>(lds, ws, w, lane, rt, cg, bi, x, wqkv, w1, w3 STAMP_ARG);
  else k1_compute<2>(lds, ws, w, lane, rt, cg, bi, x, wqkv, w1, w3 STAMP_ARG);
}

#define K2_DLY 64
#define K2D 5
constexpr int K2_D = K2D;
constexpr int K2_RED = 4 * K2_D * 4096;
constexpr int K2_LDS = K2_RED + 4 * 64 * 272 + 16;
#define K2OD 5
#define K2GO 5
#define NREP 8
constexpr int K2_GOW = K2_RED + 4 * 64 * 272;
constexpr int K2_OD = K2OD;
constexpr int K2_NPROD = 32;
constexpr int K2_NI = 36;

struct K2State {
  const char* bH;
  const char* bO;
  const char* bW2;
  const char* bWO;
  unsigned oA;
  unsigned oW[2];
  unsigned oWl;
  lds_char_ptr go;
};

constexpr int k2_no(int Q) { return (Q + K2_OD >= 28 && Q + K2_OD < K2_NI) ? 2 : 0; }
constexpr int k2_nw(int Q) { return Q + K2_D >= K2_NI ? 0 : (Q + K2_D < 28 ? 4 : 2); }
constexpr int k2_after(int P) {
  int n = 0;
  if (P + 1 < 28 || K2_OD == K2_D) {
    for (int q = P + 2 - K2_D; q <= P - 1; ++q) n += (q >= 0 ? k2_no(q) : 0) + k2_nw(q);
  } else {
    n = k2_nw(P + 1 - K2_OD);
    for (int q = P + 2 - K2_OD; q <= P - 1; ++q) n += k2_no(q) + k2_nw(q);
  }
  return n;
}

template <int P>
DI void k2_issue(const K2State& st, lds_char_ptr ring) {
  if constexpr (P < K2_NI) {
    lds_char_ptr sb = ring + (P % K2_D) * 4096;
    if constexpr (P < 28) {
      glds16s(st.oA, st.bH + P * 1024, sb);
      glds16s(st.oA, st.bH + 112 * 1024 + P * 1024, sb + 1024);
      glds16s(st.oW[0], st.bW2 + P * (16 * D_ * 2), sb + 2048);
      glds16s(st.oW[1], st.bW2 + P * (16 * D_ * 2), sb + 3072);
    } else {
      constexpr int I = P - 28, hd = I >> 1, s = I & 1;
      glds16s(st.oW[0], st.bWO + (hd * 28 + s * 16) * (D_ * 2), sb + 2048);
      glds16s(I == 7 ? st.oWl : st.oW[1], st.bWO + (hd * 28 + s * 16) * (D_ * 2), sb + 3072);
    }
  }
}
template <int P>
DI void k2_issue_o(const K2State& st, lds_char_ptr ring) {
  if constexpr (P >= 28 && P < K2_NI) {
    lds_char_ptr sb = ring + (P % K2_D) * 4096;
    glds16s_sc1(st.oA, st.bO + (P - 28) * 1024, sb);
    glds16s_sc1(st.oA, st.bO + 32768 + (P - 28) * 1024, sb + 1024);
  }
}

struct K2Frag { bf16x8 a[2]; bf16x8 w[2]; };

template <int P>
DI void k2_read(K2Frag& f, lds_char_ptr ring, int lane, int waddr) {
  lds_char_ptr sb = ring + (P % K2_D) * 4096;
  f.a[0] = ldsread16(sb + lane * 16);
  f.a[1] = ldsread16(sb + 1024 + lane * 16);
#pragma unroll
  for (int nt = 0; nt < 2; ++nt) {
    s16x4 lo = trread(sb + 2048 + waddr + nt * 256);
    s16x4 hi = trread(sb + 2048 + waddr + 1024 + nt * 256);
    f.w[nt] = cat8(lo, hi);
  }
}
DI void k2_mma(f32x16 (&acc)[2][2], const K2Frag& f) {
#pragma unroll
  for (int nt = 0; nt < 2; ++nt)
#pragma unroll
    for (int mt = 0; mt < 2; ++mt) acc[nt][mt] = MFMA(f.w[nt], f.a[mt], acc[nt][mt]);
}

template <int P>
DI void k2_step(f32x16 (&acc)[2][2], const float (&og)[2], K2Frag& fc, K2Frag& fn, const K2State& st, lds_char_ptr ring, int lane,
                int waddr STAMP_PARAM) {
  if constexpr (P + 1 < K2_NI) {
    wait_vm<k2_after(P)>();
    STAMP_NOW(3 + P);
    k2_read<P + 1>(fn, ring, lane, waddr);
    __builtin_amdgcn_sched_barrier(0);
  }
  if constexpr (P == 28) {
#pragma unroll
    for (int nt = 0; nt < 2; ++nt)
#pragma unroll
      for (int mt = 0; mt < 2; ++mt)
#pragma unroll
        for (int k = 0; k < 16; ++k) acc[nt][mt][k] *= og[mt];
  }
  k2_mma(acc, fc);
  if constexpr (P + K2_OD + K2GO == 28) *(volatile __attribute__((address_space(3))) unsigned*)(st.go) = 1u;
  if constexpr (P + K2_OD == 28) {
    __builtin_amdgcn_s_barrier();
    asm volatile("" ::: "memory");
  }
  k2_issue_o<P + K2_OD>(st, ring);
  k2_issue<P + K2_D>(st, ring);
  if constexpr (P + 1 < K2_NI) k2_step<P + 1>(acc, og, fn, fc, st, ring, lane, waddr STAMP_ARG);
}

__global__ __launch_bounds__(512, 2) void k2_kernel(const u16* __restrict__ wo, const u16* __restrict__ w2, char* __restrict__ ws,
                                                    u16* __restrict__ out) {
  __shared__ __attribute__((aligned(16))) char smem[K2_LDS];
  lds_char_ptr lds = (lds_char_ptr)smem;
  const int tid = threadIdx.x, lane = tid & 63;
  const int wv = __builtin_amdgcn_readfirstlane(tid >> 6);
  const int w = wv & 3;
  const int b = blockIdx.x;
  const int hh = lane >> 5;
  STAMP_DECL
  STAMP_NOW(0);

  asm volatile("" ::"s"(wo), "s"(w2), "s"(ws));
  if (__builtin_expect(b < K2_NPROD, 1)) {
    const int h = b >> 1, hf = b & 1;
    const int ks = wv & 1, rr = lane & 31;
    const int e = rr < 28 ? rr + 4 : 0;
    const char* src = ws + WS_KVP + ((size_t)(4 * (wv >> 1)) * 16 + h) * 4096 + (2 * ks) * 1024 + (hh * 32 + e) * 16;
    f32x4 v0[4], v1[4];
#pragma unroll
    for (int rt = 0; rt < 4; ++rt) {
      v0[rt] = *(const f32x4*)(src + (size_t)rt * 65536);
      v1[rt] = *(const f32x4*)(src + (size_t)rt * 65536 + 1024);
    }
    const int strip0 = hf * 32 + wv * 4;
    u32x4 pq[4][2];
    float g[4];
#pragma unroll
    for (int i = 0; i < 4; ++i) {
      const char* pp = ws + WS_PQ + ((size_t)(strip0 + i) * 16 + h) * 2048 + lane * 16;
      pq[i][0] = *(const u32x4*)pp;
      pq[i][1] = *(const u32x4*)(pp + 1024);
      g[i] = *(const float*)(ws + WS_G + ((strip0 + i) * 32 + rr) * 4);
    }
    STAMP_NOW(2);
    f32x4 s0 = v0[0], s1 = v1[0];
#pragma unroll
    for (int rt = 1; rt < 4; ++rt) { s0 += v0[rt]; s1 += v1[rt]; }
    *(__attribute__((address_space(3))) f32x4*)(lds + wv * 2048 + lane * 16) = s0;
    *(__attribute__((address_space(3))) f32x4*)(lds + wv * 2048 + 1024 + lane * 16) = s1;
    STAMP_NOW(3);
    wg_barrier();
    STAMP_NOW(4);
    bf16x8 kvf[2];
#pragma unroll
    for (int k2 = 0; k2 < 2; ++k2) {
      f32x4 t0 = {0.f, 0.f, 0.f, 0.f}, t1 = {0.f, 0.f, 0.f, 0.f};
#pragma unroll
      for (int q = 0; q < 4; ++q) {
        t0 += *(__attribute__((address_space(3))) f32x4*)(lds + (k2 + 2 * q) * 2048 + lane * 16);
        t1 += *(__attribute__((address_space(3))) f32x4*)(lds + (k2 + 2 * q) * 2048 + 1024 + lane * 16);
      }
      const bool row_ok = rr <= 28;
      const bool hi_ok = row_ok && !(k2 == 1 && hh == 1);
      u32x4 pk = {row_ok ? pack2(t0[0], t0[1]) : 0u, row_ok ? pack2(t0[2], t0[3]) : 0u, hi_ok ? pack2(t1[0], t1[1]) : 0u,
                  hi_ok ? pack2(t1[2], t1[3]) : 0u};
      kvf[k2] = __builtin_bit_cast(bf16x8, pk);
    }
    f32x16 num[4];
#pragma unroll
    for (int i = 0; i < 4; ++i) {
#pragma unroll
      for (int k = 0; k < 16; ++k) num[i][k] = 0.f;
      num[i] = MFMA(kvf[0], __builtin_bit_cast(bf16x8, pq[i][0]), num[i]);
    }
#pragma unroll
    for (int i = 0; i < 4; ++i) num[i] = MFMA(kvf[1], __builtin_bit_cast(bf16x8, pq[i][1]), num[i]);
#pragma unroll
    for (int i = 0; i < 4; ++i) {
      const unsigned d12 = __float_as_uint(num[i][12]);
      const auto sw = __builtin_amdgcn_permlane32_swap(d12, d12, false, false);
      float den = __uint_as_float(sw[1]);
      den = bf16r(bf16r(den) + 1e-6f);
      const float r1 = g[i] * fast_rcp(den), rz = hh ? 0.f : r1;
      float ov[16];
#pragma unroll
      for (int k = 0; k < 16; ++k) ov[k] = num[i][k] * (k >= 12 ? rz : r1);
      u32x4 o0 = {pack2(ov[0], ov[1]), pack2(ov[2], ov[3]), pack2(ov[4], ov[5]), pack2(ov[6], ov[7])};
      u32x4 o1 = {pack2(ov[8], ov[9]), pack2(ov[10], ov[11]), pack2(ov[12], ov[13]), pack2(ov[14], ov[15])};
      char* dst = ws + WS_OF + ((size_t)(strip0 + i) * 16 + h) * 2048 + lane * 16;
      asm volatile("global_store_dwordx4 %0, %1, off sc1\n\ts_nop 1" ::"v"(dst), "v"(o0) : "memory");
      asm volatile("global_store_dwordx4 %0, %1, off offset:1024 sc1\n\ts_nop 1" ::"v"(dst), "v"(o1) : "memory");
    }
    STAMP_NOW(5);
    asm volatile("s_waitcnt vmcnt(0)" ::: "memory");
    STAMP_NOW(6);
    __syncthreads();
    if (tid < NREP) __hip_atomic_fetch_add((gu32*)(ws + WS_CNT + tid * 256), 1u, RLX_AGENT);
    STAMP_NOW(1);
    if (wv < 4) STAMP_OUT(ws, 1, 7);
    return;
  }

  const int bb = b - K2_NPROD, xcd = bb & 7, bi = bb >> 3;
  const int rt = xcd * 4 + bi / 7, ct = bi % 7;
  const int row0 = rt * 64;
  constexpr int RS = 272;

  if (__builtin_expect(wv >= 4, 0)) {
    if (wv == 4) {
      __builtin_amdgcn_s_sleep(K2_DLY);
      volatile __attribute__((address_space(3))) unsigned* go = (volatile __attribute__((address_space(3))) unsigned*)(lds + K2_GOW);
      while (__builtin_amdgcn_readfirstlane(*go) == 0u) __builtin_amdgcn_s_sleep(2);
      unsigned spins = 0;
      gu32* cnt = (gu32*)(ws + WS_CNT + (b & (NREP - 1)) * 256);
      while (__builtin_amdgcn_readfirstlane(__hip_atomic_load(cnt, RLX_AGENT)) != (unsigned)K2_NPROD && spins < (1u << 20)) {
        __builtin_amdgcn_s_sleep(8);
        ++spins;
      }
      STAMP_NOW(1);
      STAMP_OUT(ws, 3, 2);
    }
    __builtin_amdgcn_s_barrier();
  } else {
    lds_char_ptr ring = lds + w * (K2_D * 4096);
    K2State st;
    st.bH = ws + WS_HF + ((size_t)(rt * 2) * 112 + 28 * w) * 1024;
    st.bO = ws + WS_OF + ((size_t)(rt * 2) * 16 + 4 * w) * 2048;
    st.bW2 = (const char*)(w2 + (size_t)(448 * w) * D_);
    st.bWO = (const char*)(wo + (size_t)(112 * w) * D_);
    st.oA = lane * 16;
    st.go = lds + K2_GOW;
    *(volatile __attribute__((address_space(3))) unsigned*)(st.go) = 0u;
    {
      const int blk = lane >> 4, row = (lane >> 2) & 3, u = lane & 3;
      const int col = 64 * ct + 32 * (blk & 1) + 8 * u;
#pragma unroll
      for (int a = 0; a < 2; ++a) st.oW[a] = ((4 * (2 * a + (blk >> 1)) + row) * D_ + col) * 2;
      st.oWl = ((8 + row) * D_ + col) * 2;
    }
    float gg[2];
    asm volatile("global_load_dword %0, %1, off" : "=v"(gg[0]) : "v"(ws + WS_G + (row0 + (lane & 31)) * 4) : "memory");
    asm volatile("global_load_dword %0, %1, off offset:128" : "=v"(gg[1]) : "v"(ws + WS_G + (row0 + (lane & 31)) * 4) : "memory");
    k2_issue<0>(st, ring); k2_issue<1>(st, ring); k2_issue<2>(st, ring); k2_issue<3>(st, ring);
    if constexpr (K2_D > 4) k2_issue<4>(st, ring);
    if constexpr (K2_D > 5) k2_issue<5>(st, ring);
    if constexpr (K2_D > 6) k2_issue<6>(st, ring);
    if constexpr (K2_D > 7) k2_issue<7>(st, ring);

    const int waddr = hh * 512 + ((lane & 15) >> 2) * 64 + ((lane >> 4) & 1) * 32 + (lane & 3) * 8;
    f32x16 acc[2][2];
#pragma unroll
    for (int i = 0; i < 2; ++i)
#pragma unroll
      for (int j = 0; j < 2; ++j)
#pragma unroll
        for (int k = 0; k < 16; ++k) acc[i][j][k] = 0.f;
    K2Frag f0, f1;
    STAMP_NOW(1);
    wait_vm<4 * (K2_D - 1)>();
    STAMP_NOW(2);
    asm volatile("" : "+v"(gg[0]), "+v"(gg[1]));
    const float og[2] = {bf16r(1.f - gg[0]), bf16r(1.f - gg[1])};
    k2_read<0>(f0, ring, lane, waddr);
    k2_step<0>(acc, og, f0, f1, st, ring, lane, waddr STAMP_ARG);

    STAMP_NOW(38);
    lds_char_ptr red = lds + K2_RED + w * (64 * RS);
#pragma unroll
    for (int mt = 0; mt < 2; ++mt) {
#pragma unroll
      for (int nt = 0; nt < 2; ++nt)
#pragma unroll
        for (int q = 0; q < 4; ++q) {
          f32x4 v;
#pragma unroll
          for (int i = 0; i < 4; ++i) v[i] = acc[nt][mt][4 * q + i];
          *(__attribute__((address_space(3))) f32x4*)(red + (32 * mt + (lane & 31)) * RS + (32 * nt + 8 * q + 4 * hh) * 4) = v;
        }
    }
  }
  wg_barrier();
  {
    const int m = tid >> 3, n0 = (tid & 7) * 8;
    f32x4 s[2];
#pragma unroll
    for (int i = 0; i < 2; ++i) s[i] = *(__attribute__((address_space(3))) f32x4*)(lds + K2_RED + m * RS + (n0 + 4 * i) * 4);
#pragma unroll
    for (int ww = 1; ww < 4; ++ww)
#pragma unroll
      for (int i = 0; i < 2; ++i) s[i] += *(__attribute__((address_space(3))) f32x4*)(lds + K2_RED + ww * (64 * RS) + m * RS + (n0 + 4 * i) * 4);
    u32x4 o0 = {pack2(s[0][0], s[0][1]), pack2(s[0][2], s[0][3]), pack2(s[1][0], s[1][1]), pack2(s[1][2], s[1][3])};
    *(u32x4*)(out + (size_t)(row0 + m) * D_ + 64 * ct + n0) = o0;
  }
  STAMP_NOW(39);
  if (wv < 4) STAMP_OUT(ws, 1, 40);
}

__device__ __attribute__((used, noinline)) void code_end_marker() { asm volatile("k2_code_mark:"); }

extern "C" void kernel_launch(void* const* d_in, const int* in_sizes, int n_in, void* d_out, int out_size, void* d_ws, size_t ws_size,
                              hipStream_t stream) {
  const u16* x = (const u16*)d_in[0];
  const u16* wqkv = (const u16*)d_in[1];
  const u16* wo = (const u16*)d_in[2];
  const u16* w1 = (const u16*)d_in[4];
  const u16* w3 = (const u16*)d_in[5];
  const u16* w2 = (const u16*)d_in[6];
  const u16* gw = (const u16*)d_in[7];
  char* ws = (char*)d_ws;
  k1_kernel<<<256, 768, 0, stream>>>(x, wqkv, w1, w3, gw, w2, wo, ws, (const u16*)d_out);
  k2_kernel<<<256, 512, 0, stream>>>(wo, w2, ws, (u16*)d_out);
}
```

```cpp
#include <hip/hip_runtime.h>
#include <hip/hip_bf16.h>
#include <stdint.h>

typedef __attribute__((ext_vector_type(8))) short bf16x8;
typedef __attribute__((ext_vector_type(4))) short s16x4;
typedef __attribute__((ext_vector_type(16))) float f32x16;
typedef __attribute__((ext_vector_type(4))) float f32x4;
typedef __attribute__((ext_vector_type(4))) unsigned u32x4;
typedef __attribute__((ext_vector_type(2))) unsigned u32x2;
typedef __attribute__((address_space(3))) s16x4* lds_s16x4_ptr;
typedef __attribute__((address_space(3))) bf16x8* lds_bf16x8_ptr;
typedef __attribute__((address_space(3))) char* lds_char_ptr;
typedef __attribute__((address_space(1))) unsigned gu32;
typedef unsigned short u16;

#define DI __device__ __forceinline__
#define MFMA(a, b, c) __builtin_amdgcn_mfma_f32_32x32x16_bf16((a), (b), (c), 0, 0, 0)
#define RLX_AGENT __ATOMIC_RELAXED, __HIP_MEMORY_SCOPE_AGENT

constexpr int D_ = 448, F_ = 1792, NQKV = 1344;

constexpr size_t WS_HF = 0;
constexpr size_t WS_PQ = WS_HF + (size_t)64 * 112 * 1024;
constexpr size_t WS_KVP = WS_PQ + (size_t)64 * 16 * 2048;
constexpr size_t WS_OF = WS_KVP + (size_t)16 * 16 * 4096;
constexpr size_t WS_G = WS_OF + (size_t)64 * 16 * 2048;
constexpr size_t WS_CNT = WS_G + 8192;

template <int N> DI void wait_vm() { asm volatile("s_waitcnt vmcnt(%0)" ::"n"(N) : "memory"); }
DI void wait_lgkm0() { asm volatile("s_waitcnt lgkmcnt(0)" ::: "memory"); }

typedef __attribute__((ext_vector_type(2))) __bf16 bf16x2v;
typedef __attribute__((ext_vector_type(2))) float f32x2;
DI unsigned pack2(float lo, float hi) {
  f32x2 f = {lo, hi};
  bf16x2v v = __builtin_convertvector(f, bf16x2v);
  return __builtin_bit_cast(unsigned, v);
}
DI float bf16r(float x) { return (float)(__bf16)x; }
DI void glds16s(unsigned voff, const void* sbase, lds_char_ptr l) {
  const unsigned la = __builtin_amdgcn_readfirstlane((unsigned)(uintptr_t)l);
  asm volatile("s_mov_b32 m0, %2\n\ts_nop 0\n\tglobal_load_lds_dwordx4 %0, %1" ::"v"(voff), "s"(sbase), "s"(la));
}
DI void glds16s_sc1(unsigned voff, const void* sbase, lds_char_ptr l) {
  const unsigned la = __builtin_amdgcn_readfirstlane((unsigned)(uintptr_t)l);
  asm volatile("s_mov_b32 m0, %2\n\ts_nop 0\n\tglobal_load_lds_dwordx4 %0, %1 sc1" ::"v"(voff), "s"(sbase), "s"(la));
}
DI void l2touch(const void* g, lds_char_ptr junk) {
  const unsigned la = __builtin_amdgcn_readfirstlane((unsigned)(uintptr_t)junk);
  asm volatile("s_mov_b32 m0, %1\n\ts_nop 0\n\tglobal_load_lds_dword %0, off" ::"v"(g), "s"(la));
}
DI void gstore16(void* p, u32x4 v) { asm volatile("global_store_dwordx4 %0, %1, off sc1\n\ts_nop 1" ::"v"(p), "v"(v) : "memory"); }
DI s16x4 trread(lds_char_ptr p) { return __builtin_amdgcn_ds_read_tr16_b64_v4i16((lds_s16x4_ptr)p); }
DI bf16x8 ldsread16(lds_char_ptr p) { return *(lds_bf16x8_ptr)p; }
DI bf16x8 cat8(s16x4 lo, s16x4 hi) { return __builtin_shufflevector(lo, hi, 0, 1, 2, 3, 4, 5, 6, 7); }
DI float fast_exp(float x) { return __builtin_amdgcn_exp2f(x * 1.4426950408889634f); }
DI float fast_rcp(float x) { return __builtin_amdgcn_rcpf(x); }
DI float phi(float t) {
  const float e = __builtin_amdgcn_fmed3f(__builtin_amdgcn_exp2f(t * 1.4426950408889634f), 0.f, 1.f);
  return (fmaxf(t, 0.f) + 1e-6f) + e;
}


enum { T_F = 0, T_Q = 1, T_K = 2, T_V = 3 };

DI void wg_barrier() {
  asm volatile("s_waitcnt lgkmcnt(0)" ::: "memory");
  __builtin_amdgcn_s_barrier();
  asm volatile("" ::: "memory");
}

DI void tile_info(int cg, int t, int& type, int& idx) {
  if (cg < 14) {
    if (t < 8) { type = T_F; idx = cg * 8 + t; }
    else if (cg < 8) { type = (t == 8) ? T_K : T_V; idx = cg; }
    else { type = T_Q; idx = 2 * (cg - 8) + (t - 8); }
  } else {
    int c = t >= 5 ? 1 : 0, tl = t - 5 * c;
    if (tl == 0) { type = T_Q; idx = 12 + 2 * (cg - 14) + c; }
    else { type = (tl & 1) ? T_K : T_V; idx = 8 + 4 * (cg - 14) + 2 * c + ((tl - 1) >> 1); }
  }
}
DI int qkv_col(int cg, int t) {
  int type, idx;
  if (cg < 8) { type = (t == 8) ? T_K : T_V; idx = cg; }
  else if (cg < 14) { type = T_Q; idx = 2 * (cg - 8) + (t - 8); }
  else {
    const int c = t >= 5 ? 1 : 0, tl = t - 5 * c;
    type = tl == 0 ? T_Q : ((tl & 1) ? T_K : T_V);
    idx = tl == 0 ? 12 + 2 * (cg - 14) + c : 8 + 4 * (cg - 14) + 2 * c + ((tl - 1) >> 1);
  }
  return idx * 28 + (type == T_Q ? 0 : (type == T_K ? 448 : 892));
}

constexpr int K1_XOFF = 0;
constexpr int K1_WOFF = 3 * 16384;
constexpr int K1_WSLOT = 20480;
constexpr int K1_JUNK = K1_WOFF + 4 * K1_WSLOT;
constexpr int K1_FLAG = K1_JUNK + 256;
constexpr int K1_LDS = K1_FLAG + 16;
constexpr int K1_WARM = 12;

struct K1State {
  const char* bF[2];
  const char* bE;
  const char* bX;
  unsigned oF[4], oE, oX[2];
  int strideF;
  int rot;
  const char* warm[2];
  const char* gx;
  const char* gg;
};
struct K1Gate { u32x4 xv[7], gv[7]; };
DI int k1_chunk(const K1State& st, int c) { const int ch = c + st.rot; return ch >= 7 ? ch - 7 : ch; }

template <int I>
DI void k1_issue(const K1State& st, lds_char_ptr lds, int w) {
  lds_char_ptr wb = lds + K1_WOFF + (I & 3) * K1_WSLOT;
  const int kb = 2 * k1_chunk(st, I >> 1) + (I & 1);
#pragma unroll
  for (int j = 0; j < 4; ++j) glds16s(st.oF[j], st.bF[j & 1] + kb * st.strideF, wb + (4 * w + j) * 1024);
  glds16s(st.oE, st.bE + kb * (32 * NQKV * 2), wb + 16384 + w * 1024);
}
template <int J>
DI void k1_issue_x(const K1State& st, lds_char_ptr lds, int w) {
  lds_char_ptr xb = lds + K1_XOFF + ((J >> 1) % 3) * 16384 + (J & 1) * 8192;
#pragma unroll
  for (int j = 0; j < 2; ++j) glds16s(st.oX[j], st.bX + (J & 1) * 64 * D_ * 2 + k1_chunk(st, J >> 1) * 128, xb + (2 * w + j) * 1024);
}
template <int I>
DI void k1_issue_group(const K1State& st, lds_char_ptr lds, int w) {
  if constexpr (I < 14) {
    k1_issue<I>(st, lds, w);
    if constexpr (I + 1 < 14) k1_issue_x<I + 1>(st, lds, w);
  }
}

template <int I>
DI void k1_loader_step(const K1State& st, lds_char_ptr lds, int lw, K1Gate& gt) {
  constexpr int outstanding = ((I == 0) ? 7 : (I <= 10) ? 14 : (I == 11 ? 12 : (I == 12 ? 5 : 0))) + (I > K1_WARM ? (I > K1_WARM + 1 ? 2 : 1) : 0);
  wait_vm<outstanding>();
  wg_barrier();
  if constexpr (I == 0) { k1_issue_group<2>(st, lds, lw); k1_issue_group<3>(st, lds, lw); }
  else k1_issue_group<I + 3>(st, lds, lw);
  if constexpr (I == K1_WARM - 1) {
    if (lw == 0) {
#pragma unroll
      for (int i = 0; i < 7; ++i) {
        asm volatile("global_load_dwordx4 %0, %1, off offset:%2" : "+v"(gt.xv[i]) : "v"(st.gx), "n"(i * 16));
        asm volatile("global_load_dwordx4 %0, %1, off offset:%2" : "+v"(gt.gv[i]) : "v"(st.gg), "n"(i * 16));
      }
    }
  }
  if constexpr (I == K1_WARM) l2touch(st.warm[0], lds + K1_JUNK);
  if constexpr (I == K1_WARM + 1) l2touch(st.warm[1], lds + K1_JUNK);
  if constexpr (I + 1 < 14) k1_loader_step<I + 1>(st, lds, lw, gt);
}

template <int NT> struct K1Frag { bf16x8 x[2]; bf16x8 w[NT]; };
template <int NT> struct K1Addr { int x; int w[NT][4]; };

template <int NT, int I, int KS>
DI void k1_read(K1Frag<NT>& f, lds_char_ptr lds, const K1Addr<NT>& ad) {
  constexpr int kk = (I & 1) * 4 + KS * 2;
  lds_char_ptr xb = lds + K1_XOFF + ((I >> 1) % 3) * 16384;
  f.x[0] = ldsread16(xb + (ad.x ^ (kk << 4)));
  f.x[1] = ldsread16(xb + (ad.x ^ (kk << 4)) + 4096);
  lds_char_ptr wb = lds + K1_WOFF + (I & 3) * K1_WSLOT;
#pragma unroll
  for (int t = 0; t < NT; ++t) {
    s16x4 lo, hi;
    if constexpr (NT == 3) {
      lo = trread(wb + ad.w[t][0] + (4 * KS) * 2048);
      hi = trread(wb + ad.w[t][0] + (4 * KS + 1) * 2048);
    } else {
      lo = trread(wb + ad.w[t][2 * KS]);
      hi = trread(wb + ad.w[t][2 * KS + 1]);
    }
    f.w[t] = cat8(lo, hi);
  }
}
template <int NT>
DI void k1_mma(f32x16 (&acc)[NT][2], const K1Frag<NT>& f) {
#pragma unroll
  for (int t = 0; t < NT; ++t) {
    acc[t][0] = MFMA(f.w[t], f.x[0], acc[t][0]);
    acc[t][1] = MFMA(f.w[t], f.x[1], acc[t][1]);
  }
}

template <int NT, int I>
DI void k1_step(f32x16 (&acc)[NT][2], K1Frag<NT>& fa, K1Frag<NT>& fb, lds_char_ptr lds, const K1Addr<NT>& ad) {
  wg_barrier();
  k1_read<NT, I, 0>(fa, lds, ad);
  if constexpr (I > 0) k1_mma<NT>(acc, fb);
  k1_read<NT, I, 1>(fb, lds, ad);
  k1_mma<NT>(acc, fa);
  if constexpr (I + 1 < 14) k1_step<NT, I + 1>(acc, fa, fb, lds, ad);
}

template <int NT>
DI void k1_compute(lds_char_ptr lds, char* __restrict__ ws, int w, int lane, int rt, int cg) {
  const int r = w & 1, c = (w >> 1) & 1;
  const int t0 = 5 * c + (NT == 3 ? 0 : 3);
  const int hh = lane >> 5, m = lane & 31;
  const int q4 = (lane & 15) >> 2, p4 = lane & 3, blk2 = (lane >> 4) & 1;
  const int xrow = 64 * r + m;
  K1Addr<NT> ad;
  ad.x = xrow * 128 + ((hh ^ ((xrow >> 1) & 7)) << 4);
  const int inblk = q4 * 64 + blk2 * 32 + p4 * 8;
#pragma unroll
  for (int t = 0; t < NT; ++t) {
    const int ts = t0 + t;
    int off, kst;
    if (ts >= 8) { off = 16384 + (ts - 8) * 256 + inblk; kst = 512; }
    else if (cg >= 14) { off = ts * 256 + inblk; kst = 2048; }
    else { off = ((ts >> 1) + 4 * blk2) * 256 + q4 * 64 + (((ts & 1) ^ blk2) * 32) + p4 * 8; kst = 2048; }
#pragma unroll
    for (int i = 0; i < 4; ++i) ad.w[t][i] = off + (4 * (i >> 1) + (i & 1) + 2 * hh) * kst;
  }

  f32x16 acc[NT][2];
#pragma unroll
  for (int t = 0; t < NT; ++t)
#pragma unroll
    for (int s = 0; s < 2; ++s)
#pragma unroll
      for (int i = 0; i < 16; ++i) acc[t][s][i] = 0.f;
  K1Frag<NT> fa, fb;
  if (w == 0 && lane < 4) *((__attribute__((address_space(3))) unsigned*)(lds + K1_FLAG) + lane) = 0u;
  k1_step<NT, 0>(acc, fa, fb, lds, ad);
  k1_mma<NT>(acc, fb);

  const int strip0 = rt * 4 + r * 2;
  {
    int type, idx; tile_info(cg, t0 + NT - 2, type, idx);
    if (type == T_K) {
      __builtin_amdgcn_s_setprio(3);
      lds_char_ptr img = lds + K1_WOFF + 2 * K1_WSLOT + w * 4096;
      constexpr int tk = NT - 2, tv = NT - 1;
      f32x16 kv;
#pragma unroll
      for (int i = 0; i < 16; ++i) kv[i] = 0.f;
#pragma unroll
      for (int s = 0; s < 2; ++s) {
#pragma unroll
        for (int g = 0; g < 4; ++g) {
          u32x2 pk = {pack2(phi(acc[tk][s][4 * g]), phi(acc[tk][s][4 * g + 1])), pack2(phi(acc[tk][s][4 * g + 2]), phi(acc[tk][s][4 * g + 3]))};
          float v0 = acc[tv][s][4 * g];
          if (g == 0) v0 = hh == 0 ? 1.f : v0;
          u32x2 vv = {pack2(v0, acc[tv][s][4 * g + 1]), pack2(acc[tv][s][4 * g + 2], acc[tv][s][4 * g + 3])};
          const int off = m * 64 + (((2 * g + hh) ^ ((m >> 1) & 7)) << 3);
          *(__attribute__((address_space(3))) u32x2*)(img + off) = pk;
          *(__attribute__((address_space(3))) u32x2*)(img + 2048 + off) = vv;
        }
#pragma unroll
        for (int ks = 0; ks < 2; ++ks) {
          const int r0 = 16 * ks + 8 * hh + q4, r1 = r0 + 4, un = 4 * blk2 + p4;
          const int off0 = r0 * 64 + ((un ^ ((r0 >> 1) & 7)) << 3), off1 = r1 * 64 + ((un ^ ((r1 >> 1) & 7)) << 3);
          bf16x8 a = cat8(trread(img + off0), trread(img + off1));
          bf16x8 bb = cat8(trread(img + 2048 + off0), trread(img + 2048 + off1));
          kv = MFMA(a, bb, kv);
        }
      }
      lds_char_ptr xch = lds + K1_XOFF + 16384 + (w >> 1) * 4096;
      volatile __attribute__((address_space(3))) unsigned* flag = (volatile __attribute__((address_space(3))) unsigned*)(lds + K1_FLAG) + (w >> 1);
      if (r == 1) {
#pragma unroll
        for (int g = 0; g < 4; ++g) {
          f32x4 v = {kv[4 * g], kv[4 * g + 1], kv[4 * g + 2], kv[4 * g + 3]};
          *(__attribute__((address_space(3))) f32x4*)(xch + g * 1024 + lane * 16) = v;
        }
        wait_lgkm0();
        *flag = 1u;
      } else {
        while (__builtin_amdgcn_readfirstlane(*flag) == 0u) __builtin_amdgcn_s_sleep(1);
        asm volatile("" ::: "memory");
#pragma unroll
        for (int g = 0; g < 4; ++g) {
          f32x4 v = *(__attribute__((address_space(3))) f32x4*)(xch + g * 1024 + lane * 16);
          v[0] += kv[4 * g]; v[1] += kv[4 * g + 1]; v[2] += kv[4 * g + 2]; v[3] += kv[4 * g + 3];
          gstore16(ws + WS_KVP + ((size_t)(rt * 16 + idx) * 4 + g) * 1024 + lane * 16, __builtin_bit_cast(u32x4, v));
        }
      }
      __builtin_amdgcn_s_setprio(0);
    }
  }
#pragma unroll
  for (int t = 0; t < NT; ++t) {
    int type, idx; tile_info(cg, t0 + t, type, idx);
    if (type == T_F) {
#pragma unroll
      for (int s = 0; s < 2; ++s) {
        float hv[8];
#pragma unroll
        for (int j = 0; j < 8; ++j) {
          const float gt = acc[t][s][j], up = acc[t][s][8 + j];
          hv[j] = gt * up * fast_rcp(1.f + fast_exp(-gt));
        }
        u32x4 o = {pack2(hv[0], hv[1]), pack2(hv[2], hv[3]), pack2(hv[4], hv[5]), pack2(hv[6], hv[7])};
        gstore16(ws + WS_HF + ((size_t)(strip0 + s) * 112 + idx) * 1024 + lane * 16, o);
      }
    } else if (type == T_Q) {
#pragma unroll
      for (int s = 0; s < 2; ++s) {
        float pv[16];
#pragma unroll
        for (int j = 0; j < 16; ++j) pv[j] = phi(acc[t][s][j]);
        if (hh) { pv[12] = 0.f; pv[13] = 0.f; pv[14] = 0.f; pv[15] = 0.f; }
        u32x4 o0 = {pack2(pv[0], pv[1]), pack2(pv[2], pv[3]), pack2(pv[4], pv[5]), pack2(pv[6], pv[7])};
        u32x4 o1 = {pack2(pv[8], pv[9]), pack2(pv[10], pv[11]), pack2(pv[12], pv[13]), pack2(pv[14], pv[15])};
        char* dst = ws + WS_PQ + ((size_t)(strip0 + s) * 16 + idx) * 2048 + lane * 16;
        gstore16(dst, o0);
        gstore16(dst + 1024, o1);
      }
    }
  }
}

__global__ __launch_bounds__(768, 3) void k1_kernel(const u16* __restrict__ x, const u16* __restrict__ wqkv, const u16* __restrict__ w1,
                                                    const u16* __restrict__ w3, const u16* __restrict__ gw, const u16* __restrict__ w2,
                                                    const u16* __restrict__ wo, char* __restrict__ ws) {
  __shared__ __attribute__((aligned(16))) char smem[K1_LDS];
  lds_char_ptr lds = (lds_char_ptr)smem;
  const int tid = threadIdx.x, lane = tid & 63;
  const int w = __builtin_amdgcn_readfirstlane(tid >> 6);
  const int b = blockIdx.x, xcd = b & 7, bi = b >> 3;
  const int rt = (xcd & 1) * 8 + (bi & 7);
  const int cg = (xcd >> 1) * 4 + (bi >> 3);

  asm volatile("" ::"s"(x), "s"(wqkv), "s"(w1), "s"(w3));
  if (__builtin_expect(w >= 8, 1)) {
    const int lw = w - 8;
    K1State st;
    const int blk = lane >> 4, row = (lane >> 2) & 3, u = lane & 3;
    st.bX = (const char*)(x + (size_t)rt * 128 * D_);
    st.rot = ((bi & 7) + 2 * (bi >> 3)) % 7;
#pragma unroll
    for (int j = 0; j < 2; ++j) {
      const int xr = 8 * (2 * lw + j) + (lane >> 3), pos = lane & 7, ch = pos ^ ((xr >> 1) & 7);
      st.oX[j] = (xr * D_ + ch * 8) * 2;
    }
    k1_issue_x<0>(st, lds, lw);
    st.bE = (const char*)wqkv;
    if (cg < 14) {
      st.strideF = 32 * F_ * 2;
      st.bF[0] = (const char*)w1; st.bF[1] = (const char*)w3;
#pragma unroll
      for (int j = 0; j < 4; ++j)
        st.oF[j] = ((4 * (2 * lw + (j >> 1)) + row) * F_ + 128 * cg + 32 * blk + 8 * ((j & 1) ? (u ^ 2) : u)) * 2;
    } else {
      st.strideF = 32 * NQKV * 2;
      st.bF[0] = (const char*)wqkv; st.bF[1] = (const char*)wqkv;
#pragma unroll
      for (int j = 0; j < 4; ++j) st.oF[j] = ((4 * (2 * lw + (j >> 1)) + row) * NQKV + qkv_col(cg, 4 * (j & 1) + blk) + 8 * u) * 2;
    }
    st.oE = ((4 * (2 * lw + (blk >> 1)) + row) * NQKV + qkv_col(cg, 8 + (blk & 1)) + 8 * u) * 2;
    k1_issue_group<0>(st, lds, lw);
    k1_issue_group<1>(st, lds, lw);
    {
      const int l2 = bi * 392 + lw * 98;
      int lo = bi * 98 + lw * 25 + (lane - 34);
      lo = lo < 3136 ? lo : 3135;
      st.warm[0] = (const char*)w2 + (size_t)(l2 + lane) * 128;
      st.warm[1] = lane < 34 ? (const char*)w2 + (size_t)(l2 + 64 + lane) * 128 : (const char*)wo + (size_t)(lane < 59 ? lo : bi * 98) * 128;
    }
    const int grow = rt * 128 + cg * 8 + (lane >> 3), seg = lane & 7;
    st.gx = (const char*)(x + (size_t)grow * D_ + seg * 56);
    st.gg = (const char*)(gw + seg * 56);
    asm volatile("" : "+v"(st.gx), "+v"(st.gg), "+v"(st.warm[0]), "+v"(st.warm[1]));
    K1Gate gt;
#pragma unroll
    for (int i = 0; i < 7; ++i) { gt.xv[i] = u32x4{0u, 0u, 0u, 0u}; gt.gv[i] = u32x4{0u, 0u, 0u, 0u}; }
#pragma unroll
    for (int i = 0; i < 7; ++i) asm volatile("" : "+v"(gt.xv[i]), "+v"(gt.gv[i]));
    k1_loader_step<0>(st, lds, lw, gt);
#pragma unroll
    for (int i = 0; i < 7; ++i) asm volatile("" : "+v"(gt.xv[i]), "+v"(gt.gv[i]));
    if (lw == 0) {
      if (b == 0 && lane < 32) *(unsigned*)(ws + WS_CNT + lane * 256) = 0u;
      float gsum = 0.f;
#pragma unroll
      for (int i = 0; i < 7; ++i)
#pragma unroll
        for (int j = 0; j < 4; ++j) {
          const unsigned xe = gt.xv[i][j], ge = gt.gv[i][j];
          gsum = __builtin_amdgcn_fdot2_f32_bf16(__builtin_bit_cast(bf16x2v, xe), __builtin_bit_cast(bf16x2v, ge), gsum, false);
        }
      gsum += __shfl_xor(gsum, 1);
      gsum += __shfl_xor(gsum, 2);
      gsum += __shfl_xor(gsum, 4);
      const float gs = bf16r(gsum);
      const float g = bf16r(1.f / (1.f + __expf(-gs)));
      if (seg == 0) *(float*)(ws + WS_G + grow * 4) = g;
    }
    return;
  }
  if (w == 7) {
    const char* pc; int len;
    asm volatile("s_getpc_b64 %0\nk1_code_mark:\n\ts_mov_b32 %1, k2_code_mark-k1_code_mark" : "=s"(pc), "=s"(len));
    int off = (bi * 16 + (lane & 15)) * 128;
    off = off < len ? off : 0;
    l2touch(pc + off, lds + K1_JUNK);
  }
  if (w < 4) k1_compute<3>(lds, ws, w, lane, rt, cg);
  else k1_compute<2>(lds, ws, w, lane, rt, cg);
}

constexpr int K2_D = 5;
constexpr int K2_RED = 4 * K2_D * 4096;
constexpr int K2_LDS = K2_RED + 4 * 64 * 272 + 16;
constexpr int K2_GOW = K2_RED + 4 * 64 * 272;
constexpr int K2_OD = 5;
constexpr int K2_NPROD = 32;
constexpr int K2_NREP = 8;
constexpr int K2_GO = 5;
constexpr int K2_NI = 36;

struct K2State {
  const char* bH;
  const char* bO;
  const char* bW2;
  const char* bWO;
  unsigned oA;
  unsigned oW[2];
  unsigned oWl;
  lds_char_ptr go;
};

constexpr int k2_no(int Q) { return (Q + K2_OD >= 28 && Q + K2_OD < K2_NI) ? 2 : 0; }
constexpr int k2_nw(int Q) { return Q + K2_D >= K2_NI ? 0 : (Q + K2_D < 28 ? 4 : 2); }
constexpr int k2_after(int P) {
  int n = 0;
  if (P + 1 < 28 || K2_OD == K2_D) {
    for (int q = P + 2 - K2_D; q <= P - 1; ++q) n += (q >= 0 ? k2_no(q) : 0) + k2_nw(q);
  } else {
    n = k2_nw(P + 1 - K2_OD);
    for (int q = P + 2 - K2_OD; q <= P - 1; ++q) n += k2_no(q) + k2_nw(q);
  }
  return n;
}

template <int P>
DI void k2_issue(const K2State& st, lds_char_ptr ring) {
  if constexpr (P < K2_NI) {
    lds_char_ptr sb = ring + (P % K2_D) * 4096;
    if constexpr (P < 28) {
      glds16s(st.oA, st.bH + P * 1024, sb);
      glds16s(st.oA, st.bH + 112 * 1024 + P * 1024, sb + 1024);
      glds16s(st.oW[0], st.bW2 + P * (16 * D_ * 2), sb + 2048);
      glds16s(st.oW[1], st.bW2 + P * (16 * D_ * 2), sb + 3072);
    } else {
      constexpr int I = P - 28, hd = I >> 1, s = I & 1;
      glds16s(st.oW[0], st.bWO + (hd * 28 + s * 16) * (D_ * 2), sb + 2048);
      glds16s(I == 7 ? st.oWl : st.oW[1], st.bWO + (hd * 28 + s * 16) * (D_ * 2), sb + 3072);
    }
  }
}
template <int P>
DI void k2_issue_o(const K2State& st, lds_char_ptr ring) {
  if constexpr (P >= 28 && P < K2_NI) {
    lds_char_ptr sb = ring + (P % K2_D) * 4096;
    glds16s_sc1(st.oA, st.bO + (P - 28) * 1024, sb);
    glds16s_sc1(st.oA, st.bO + 32768 + (P - 28) * 1024, sb + 1024);
  }
}

struct K2Frag { bf16x8 a[2]; bf16x8 w[2]; };

template <int P>
DI void k2_read(K2Frag& f, lds_char_ptr ring, int lane, int waddr) {
  lds_char_ptr sb = ring + (P % K2_D) * 4096;
  f.a[0] = ldsread16(sb + lane * 16);
  f.a[1] = ldsread16(sb + 1024 + lane * 16);
#pragma unroll
  for (int nt = 0; nt < 2; ++nt) {
    s16x4 lo = trread(sb + 2048 + waddr + nt * 256);
    s16x4 hi = trread(sb + 2048 + waddr + 1024 + nt * 256);
    f.w[nt] = cat8(lo, hi);
  }
}
DI void k2_mma(f32x16 (&acc)[2][2], const K2Frag& f) {
#pragma unroll
  for (int nt = 0; nt < 2; ++nt)
#pragma unroll
    for (int mt = 0; mt < 2; ++mt) acc[nt][mt] = MFMA(f.w[nt], f.a[mt], acc[nt][mt]);
}

template <int P>
DI void k2_step(f32x16 (&acc)[2][2], const float (&og)[2], K2Frag& fc, K2Frag& fn, const K2State& st, lds_char_ptr ring, int lane,
                int waddr) {
  if constexpr (P + 1 < K2_NI) {
    wait_vm<k2_after(P)>();
    k2_read<P + 1>(fn, ring, lane, waddr);
    __builtin_amdgcn_sched_barrier(0);
  }
  if constexpr (P == 28) {
#pragma unroll
    for (int nt = 0; nt < 2; ++nt)
#pragma unroll
      for (int mt = 0; mt < 2; ++mt)
#pragma unroll
        for (int k = 0; k < 16; ++k) acc[nt][mt][k] *= og[mt];
  }
  k2_mma(acc, fc);
  __builtin_amdgcn_sched_barrier(0);
  if constexpr (P + K2_OD + K2_GO == 28) *(volatile __attribute__((address_space(3))) unsigned*)(st.go) = 1u;
  if constexpr (P + K2_OD == 28) {
    __builtin_amdgcn_s_barrier();
    asm volatile("" ::: "memory");
  }
  k2_issue_o<P + K2_OD>(st, ring);
  k2_issue<P + K2_D>(st, ring);
  if constexpr (P + 1 < K2_NI) k2_step<P + 1>(acc, og, fn, fc, st, ring, lane, waddr);
}

__global__ __launch_bounds__(512, 2) void k2_kernel(const u16* __restrict__ wo, const u16* __restrict__ w2, char* __restrict__ ws,
                                                    u16* __restrict__ out) {
  __shared__ __attribute__((aligned(16))) char smem[K2_LDS];
  lds_char_ptr lds = (lds_char_ptr)smem;
  const int tid = threadIdx.x, lane = tid & 63;
  const int wv = __builtin_amdgcn_readfirstlane(tid >> 6);
  const int w = wv & 3;
  const int b = blockIdx.x;
  const int hh = lane >> 5;

  asm volatile("" ::"s"(wo), "s"(w2), "s"(ws));
  if (__builtin_expect(b < K2_NPROD, 1)) {
    const int h = b >> 1, hf = b & 1;
    const int ks = wv & 1, rr = lane & 31;
    const int e = rr < 28 ? rr + 4 : 0;
    const char* src = ws + WS_KVP + ((size_t)(4 * (wv >> 1)) * 16 + h) * 4096 + (2 * ks) * 1024 + (hh * 32 + e) * 16;
    f32x4 v0[4], v1[4];
#pragma unroll
    for (int rt = 0; rt < 4; ++rt) {
      v0[rt] = *(const f32x4*)(src + (size_t)rt * 65536);
      v1[rt] = *(const f32x4*)(src + (size_t)rt * 65536 + 1024);
    }
    const int strip0 = hf * 32 + wv * 4;
    u32x4 pq[4][2];
    float g[4];
#pragma unroll
    for (int i = 0; i < 4; ++i) {
      const char* pp = ws + WS_PQ + ((size_t)(strip0 + i) * 16 + h) * 2048 + lane * 16;
      pq[i][0] = *(const u32x4*)pp;
      pq[i][1] = *(const u32x4*)(pp + 1024);
      g[i] = *(const float*)(ws + WS_G + ((strip0 + i) * 32 + rr) * 4);
    }
    f32x4 s0 = v0[0], s1 = v1[0];
#pragma unroll
    for (int rt = 1; rt < 4; ++rt) { s0 += v0[rt]; s1 += v1[rt]; }
    *(__attribute__((address_space(3))) f32x4*)(lds + wv * 2048 + lane * 16) = s0;
    *(__attribute__((address_space(3))) f32x4*)(lds + wv * 2048 + 1024 + lane * 16) = s1;
    wg_barrier();
    bf16x8 kvf[2];
#pragma unroll
    for (int k2 = 0; k2 < 2; ++k2) {
      f32x4 t0 = {0.f, 0.f, 0.f, 0.f}, t1 = {0.f, 0.f, 0.f, 0.f};
#pragma unroll
      for (int q = 0; q < 4; ++q) {
        t0 += *(__attribute__((address_space(3))) f32x4*)(lds + (k2 + 2 * q) * 2048 + lane * 16);
        t1 += *(__attribute__((address_space(3))) f32x4*)(lds + (k2 + 2 * q) * 2048 + 1024 + lane * 16);
      }
      const bool row_ok = rr <= 28;
      const bool hi_ok = row_ok && !(k2 == 1 && hh == 1);
      u32x4 pk = {row_ok ? pack2(t0[0], t0[1]) : 0u, row_ok ? pack2(t0[2], t0[3]) : 0u, hi_ok ? pack2(t1[0], t1[1]) : 0u,
                  hi_ok ? pack2(t1[2], t1[3]) : 0u};
      kvf[k2] = __builtin_bit_cast(bf16x8, pk);
    }
    f32x16 num[4];
#pragma unroll
    for (int i = 0; i < 4; ++i) {
#pragma unroll
      for (int k = 0; k < 16; ++k) num[i][k] = 0.f;
      num[i] = MFMA(kvf[0], __builtin_bit_cast(bf16x8, pq[i][0]), num[i]);
    }
#pragma unroll
    for (int i = 0; i < 4; ++i) num[i] = MFMA(kvf[1], __builtin_bit_cast(bf16x8, pq[i][1]), num[i]);
#pragma unroll
    for (int i = 0; i < 4; ++i) {
      const unsigned d12 = __float_as_uint(num[i][12]);
      const auto sw = __builtin_amdgcn_permlane32_swap(d12, d12, false, false);
      float den = __uint_as_float(sw[1]);
      den = bf16r(bf16r(den) + 1e-6f);
      const float r1 = g[i] * fast_rcp(den), rz = hh ? 0.f : r1;
      float ov[16];
#pragma unroll
      for (int k = 0; k < 16; ++k) ov[k] = num[i][k] * (k >= 12 ? rz : r1);
      u32x4 o0 = {pack2(ov[0], ov[1]), pack2(ov[2], ov[3]), pack2(ov[4], ov[5]), pack2(ov[6], ov[7])};
      u32x4 o1 = {pack2(ov[8], ov[9]), pack2(ov[10], ov[11]), pack2(ov[12], ov[13]), pack2(ov[14], ov[15])};
      char* dst = ws + WS_OF + ((size_t)(strip0 + i) * 16 + h) * 2048 + lane * 16;
      asm volatile("global_store_dwordx4 %0, %1, off sc1\n\ts_nop 1" ::"v"(dst), "v"(o0) : "memory");
      asm volatile("global_store_dwordx4 %0, %1, off offset:1024 sc1\n\ts_nop 1" ::"v"(dst), "v"(o1) : "memory");
    }
    asm volatile("s_waitcnt vmcnt(0)" ::: "memory");
    __syncthreads();
    if (tid < K2_NREP) __hip_atomic_fetch_add((gu32*)(ws + WS_CNT + tid * 256), 1u, RLX_AGENT);
    return;
  }

  const int bb = b - K2_NPROD, xcd = bb & 7, bi = bb >> 3;
  const int rt = xcd * 4 + bi / 7, ct = bi % 7;
  const int row0 = rt * 64;
  constexpr int RS = 272;

  if (__builtin_expect(wv >= 4, 0)) {
    volatile __attribute__((address_space(3))) unsigned* go = (volatile __attribute__((address_space(3))) unsigned*)(lds + K2_GOW);
    if (wv == 4) *go = 0u;
    wg_barrier();
    if (wv == 4) {
      __builtin_amdgcn_s_sleep(64);
      while (__builtin_amdgcn_readfirstlane(*go) == 0u) __builtin_amdgcn_s_sleep(2);
      unsigned spins = 0;
      gu32* cnt = (gu32*)(ws + WS_CNT + (b & (K2_NREP - 1)) * 256);
      while (__builtin_amdgcn_readfirstlane(__hip_atomic_load(cnt, RLX_AGENT)) != (unsigned)K2_NPROD && spins < (1u << 20)) {
        __builtin_amdgcn_s_sleep(8);
        ++spins;
      }
    }
    __builtin_amdgcn_s_barrier();
  } else {
    lds_char_ptr ring = lds + w * (K2_D * 4096);
    K2State st;
    st.bH = ws + WS_HF + ((size_t)(rt * 2) * 112 + 28 * w) * 1024;
    st.bO = ws + WS_OF + ((size_t)(rt * 2) * 16 + 4 * w) * 2048;
    st.bW2 = (const char*)(w2 + (size_t)(448 * w) * D_);
    st.bWO = (const char*)(wo + (size_t)(112 * w) * D_);
    st.oA = lane * 16;
    st.go = lds + K2_GOW;
    {
      const int blk = lane >> 4, row = (lane >> 2) & 3, u = lane & 3;
      const int col = 64 * ct + 32 * (blk & 1) + 8 * u;
#pragma unroll
      for (int a = 0; a < 2; ++a) st.oW[a] = ((4 * (2 * a + (blk >> 1)) + row) * D_ + col) * 2;
      st.oWl = ((8 + row) * D_ + col) * 2;
    }
    float gg[2];
    asm volatile("global_load_dword %0, %1, off" : "=v"(gg[0]) : "v"(ws + WS_G + (row0 + (lane & 31)) * 4) : "memory");
    asm volatile("global_load_dword %0, %1, off offset:128" : "=v"(gg[1]) : "v"(ws + WS_G + (row0 + (lane & 31)) * 4) : "memory");
    k2_issue<0>(st, ring); k2_issue<1>(st, ring); k2_issue<2>(st, ring); k2_issue<3>(st, ring);
    if constexpr (K2_D > 4) k2_issue<4>(st, ring);
    if constexpr (K2_D > 5) k2_issue<5>(st, ring);
    if constexpr (K2_D > 6) k2_issue<6>(st, ring);
    if constexpr (K2_D > 7) k2_issue<7>(st, ring);
    __builtin_amdgcn_s_barrier();

    const int waddr = hh * 512 + ((lane & 15) >> 2) * 64 + ((lane >> 4) & 1) * 32 + (lane & 3) * 8;
    f32x16 acc[2][2];
#pragma unroll
    for (int i = 0; i < 2; ++i)
#pragma unroll
      for (int j = 0; j < 2; ++j)
#pragma unroll
        for (int k = 0; k < 16; ++k) acc[i][j][k] = 0.f;
    K2Frag f0, f1;
    wait_vm<4 * (K2_D - 1)>();
    asm volatile("" : "+v"(gg[0]), "+v"(gg[1]));
    const float og[2] = {bf16r(1.f - gg[0]), bf16r(1.f - gg[1])};
    k2_read<0>(f0, ring, lane, waddr);
    k2_step<0>(acc, og, f0, f1, st, ring, lane, waddr);

    lds_char_ptr red = lds + K2_RED + w * (64 * RS);
#pragma unroll
    for (int mt = 0; mt < 2; ++mt) {
#pragma unroll
      for (int nt = 0; nt < 2; ++nt)
#pragma unroll
        for (int q = 0; q < 4; ++q) {
          f32x4 v;
#pragma unroll
          for (int i = 0; i < 4; ++i) v[i] = acc[nt][mt][4 * q + i];
          *(__attribute__((address_space(3))) f32x4*)(red + (32 * mt + (lane & 31)) * RS + (32 * nt + 8 * q + 4 * hh) * 4) = v;
        }
    }
  }
  wg_barrier();
  {
    const int m = tid >> 3, n0 = (tid & 7) * 8;
    f32x4 s[2];
#pragma unroll
    for (int i = 0; i < 2; ++i) s[i] = *(__attribute__((address_space(3))) f32x4*)(lds + K2_RED + m * RS + (n0 + 4 * i) * 4);
#pragma unroll
    for (int ww = 1; ww < 4; ++ww)
#pragma unroll
      for (int i = 0; i < 2; ++i) s[i] += *(__attribute__((address_space(3))) f32x4*)(lds + K2_RED + ww * (64 * RS) + m * RS + (n0 + 4 * i) * 4);
    u32x4 o0 = {pack2(s[0][0], s[0][1]), pack2(s[0][2], s[0][3]), pack2(s[1][0], s[1][1]), pack2(s[1][2], s[1][3])};
    gstore16(out + (size_t)(row0 + m) * D_ + 64 * ct + n0, o0);
  }
}

__device__ __attribute__((used, noinline)) void code_end_marker() { asm volatile("k2_code_mark:"); }

extern "C" void kernel_launch(void* const* d_in, const int* in_sizes, int n_in, void* d_out, int out_size, void* d_ws, size_t ws_size,
                              hipStream_t stream) {
  const u16* x = (const u16*)d_in[0];
  const u16* wqkv = (const u16*)d_in[1];
  const u16* wo = (const u16*)d_in[2];
  const u16* w1 = (const u16*)d_in[4];
  const u16* w3 = (const u16*)d_in[5];
  const u16* w2 = (const u16*)d_in[6];
  const u16* gw = (const u16*)d_in[7];
  char* ws = (char*)d_ws;
  k1_kernel<<<256, 768, 0, stream>>>(x, wqkv, w1, w3, gw, w2, wo, ws);
  k2_kernel<<<256, 512, 0, stream>>>(wo, w2, ws, (u16*)d_out);
}
```
